```python
import torch

def _softmax(x, axis=-1):
    return torch.softmax(x, dim=axis)
def _gelu(x):
    return torch.nn.functional.gelu(x, approximate="tanh")
def _silu(x):
    return torch.nn.functional.silu(x)
def _sigmoid(x):
    return torch.sigmoid(x)
def _layernorm(x, axis=-1, eps=1e-5):
    m = x.mean(dim=axis, keepdim=True)
    v = x.var(dim=axis, keepdim=True, unbiased=False)
    return (x - m) / torch.sqrt(v + eps)
def _scatter_add(tgt, idx, src, axis=0):
    return tgt.clone().index_add_(axis, idx, src)
def _segment_sum(src, seg, n):
    out = torch.zeros((n,) + src.shape[1:], dtype=src.dtype)
    return out.index_add_(0, seg, src)
def _pad(x, pw):
    flat = tuple(v for pair in reversed(pw) for v in pair)
    return torch.nn.functional.pad(x, flat)

kernel_name = 'parallel_sum__grouped_kv_mqa__swiglu_ffn__prompt'

def setup_inputs():

    _w = (lambda a: (lambda t: t.to(torch.bfloat16) if t.is_floating_point() else t)(torch.as_tensor(a)))
    def _A_setup():
        B, S, D, H = 1, 2048, 640, 32
        Dh = D // H
        return {
            "x": _w((lambda *s: torch.randn(*s, dtype=torch.bfloat16) * 0.05)(B, S, D)),
            "wq": _w((lambda *s: torch.randn(*s, dtype=torch.bfloat16) * 0.2)(D, H * Dh)),
            "wk": _w((lambda *s: torch.randn(*s, dtype=torch.bfloat16) * 0.2)(D, Dh)),
            "wv": _w((lambda *s: torch.randn(*s, dtype=torch.bfloat16) * 0.1)(D, Dh)),
            "wo": _w((lambda *s: torch.randn(*s, dtype=torch.bfloat16) * 0.1)(H * Dh, D)),
        }
    _A_in = _A_setup()

    def _B_setup():
        B, S, D, F = 1, 2048, 640, 1536
        return {
            "x": _w((lambda *s: torch.randn(*s, dtype=torch.bfloat16) * 0.05)(B, S, D)),
            "w_gate": _w((lambda *s: torch.randn(*s, dtype=torch.bfloat16) * 0.1)(D, F)),
            "w_up": _w((lambda *s: torch.randn(*s, dtype=torch.bfloat16) * 0.1)(D, F)),
            "w_down": _w((lambda *s: torch.randn(*s, dtype=torch.bfloat16) * 0.1)(F, D)),
        }
    _B_in = _B_setup()

    return {
        "A_x": _A_in["x"],
        "A_wq": _A_in["wq"],
        "A_wk": _A_in["wk"],
        "A_wv": _A_in["wv"],
        "A_wo": _A_in["wo"],
        "B_x": _B_in["x"],
        "B_w_gate": _B_in["w_gate"],
        "B_w_up": _B_in["w_up"],
        "B_w_down": _B_in["w_down"],
    }

def reference(A_x, A_wq, A_wk, A_wv, A_wo, B_x, B_w_gate, B_w_up, B_w_down):
    def _A_ref(x, wq, wk, wv, wo):
        B, S, D = x.shape
        H = 32
        Dh = D // H
        q = (lambda x, *d: x.permute(*d))((x @ wq).reshape(B, S, H, Dh), 0, 2, 1, 3)
        k = x @ wk
        v = x @ wv
        scores = torch.einsum("bhqd,bkd->bhqk", q, k) * (Dh ** -0.5)
        p = _softmax(scores, axis=-1)
        o = torch.einsum("bhqk,bkd->bhqd", p, v)
        return (lambda x, *d: x.permute(*d))(o, 0, 2, 1, 3).reshape(B, S, H * Dh) @ wo

    def _B_ref(x, w_gate, w_up, w_down):
        g = _silu(x @ w_gate)
        u = x @ w_up
        return (g * u) @ w_down

    x = A_x
    xn = _layernorm(x, axis=-1)
    return x + _A_ref(xn, A_wq, A_wk, A_wv, A_wo) + _B_ref(xn, B_w_gate, B_w_up, B_w_down)
```

```cpp
#define QD_AT 99
#include <hip/hip_runtime.h>
#include <hip/hip_bf16.h>
#include <stdint.h>
#include <type_traits>

typedef __attribute__((ext_vector_type(8))) __bf16 bf16x8;
typedef __attribute__((ext_vector_type(4))) __bf16 bf16x4;
typedef __attribute__((ext_vector_type(2))) __bf16 bf16x2;
typedef __attribute__((ext_vector_type(4))) short s16x4;
typedef __attribute__((ext_vector_type(8))) short s16x8;
typedef __attribute__((ext_vector_type(2))) float f32x2;
typedef __attribute__((ext_vector_type(4))) float f32x4;
typedef __attribute__((ext_vector_type(16))) float f32x16;
typedef __attribute__((ext_vector_type(2))) unsigned u32x2;
typedef __attribute__((ext_vector_type(4))) unsigned u32x4;
#define LDS __attribute__((address_space(3)))
#define DI __device__ __forceinline__
typedef __attribute__((address_space(1))) unsigned gu32;
#define RLX_AGENT __ATOMIC_RELAXED, __HIP_MEMORY_SCOPE_AGENT

constexpr int S_ = 2048, D_ = 640, F_ = 1536;
constexpr int KVB = 2688;
constexpr int KV_V = 1024, KV_K1 = 2368, KV_C = 2624;
constexpr size_t WS_XN = 0;
constexpr size_t WS_KV = WS_XN + (size_t)64 * 40 * 1024;
constexpr size_t WS_WQ = WS_KV + 64 * KVB;
constexpr size_t WS_WO = WS_WQ + (size_t)8 * 40 * 2560;
constexpr size_t WS_WD = WS_WO + (size_t)8 * 40 * 2560;
constexpr size_t WS_H = WS_WD + (size_t)8 * 96 * 2560;
constexpr size_t WS_O = WS_H + (size_t)64 * 96 * 1024;
constexpr size_t WS_ST = WS_O + (size_t)64 * 40 * 1024;
constexpr size_t WS_FLAG = WS_ST + 2048 * 8;
constexpr int FL_E = 0;
constexpr int FL_S = 16;
constexpr int FL_XN = FL_S + 512;
constexpr int FL_WQ = FL_XN + 128;
constexpr int FL_WO = FL_WQ + 128;
constexpr int FL_WD = FL_WO + 64;
constexpr int FL_H = FL_WD + 256;
constexpr int FL_O = FL_H + 1024;
constexpr int FL_QD = FL_O + 1024;
constexpr int FL_END = FL_QD + 256;
constexpr size_t WS_STAMP = WS_FLAG + 65536;
constexpr size_t WS_END = WS_STAMP + 1048576;
typedef __attribute__((address_space(1))) unsigned long long gu64;

DI float bflo(unsigned u) { return __uint_as_float(u << 16); }
DI float bfhi(unsigned u) { return __uint_as_float(u & 0xffff0000u); }
DI unsigned pk2(float a, float b) { f32x2 p = {a, b}; bf16x2 q = __builtin_convertvector(p, bf16x2); return __builtin_bit_cast(unsigned, q); }
DI float rbf(float a) { return bflo(pk2(a, a)); }
DI void glds_m0(unsigned lds_dst) { asm volatile("s_mov_b32 m0, %0\n\ts_nop 0" :: "s"(lds_dst) : "memory"); }
template <int OFF> DI void glds_go(const char* sbase, unsigned voff) { asm volatile("global_load_lds_dwordx4 %0, %1 offset:%2" :: "v"(voff), "s"(sbase), "i"(OFF) : "memory"); }
template <int OFF> DI void glds_go_sc1(const char* sbase, unsigned voff) { asm volatile("global_load_lds_dwordx4 %0, %1 offset:%2 sc1" :: "v"(voff), "s"(sbase), "i"(OFF) : "memory"); }
#define WAITV(n) asm volatile("s_waitcnt vmcnt(" #n ")" ::: "memory")
#define CFENCE() asm volatile("" ::: "memory")
#define WAITL0() do { CFENCE(); __builtin_amdgcn_s_waitcnt(0xC07F); } while (0)
#define SB0() __builtin_amdgcn_sched_barrier(0)
#define BAR() do { CFENCE(); __builtin_amdgcn_s_barrier(); CFENCE(); } while (0)
#define MFMA32(a, b, c) __builtin_amdgcn_mfma_f32_32x32x16_bf16((a), (b), (c), 0, 0, 0)
#define MFMA32K8(a, b, c) __builtin_amdgcn_mfma_f32_32x32x8bf16_1k((a), (b), (c), 0, 0, 0)
#define MFMA16(a, b, c) __builtin_amdgcn_mfma_f32_16x16x32_bf16((a), (b), (c), 0, 0, 0)
DI s16x4 trread(unsigned lds_addr) { return __builtin_amdgcn_ds_read_tr16_b64_v4i16((LDS s16x4*)lds_addr); }
DI bf16x8 cat8(s16x4 lo, s16x4 hi) { s16x8 v = __builtin_shufflevector(lo, hi, 0, 1, 2, 3, 4, 5, 6, 7); return __builtin_bit_cast(bf16x8, v); }
DI bf16x8 ldsr128(unsigned a) { return *(LDS bf16x8*)a; }
DI s16x4 ldsr64(unsigned a) { return *(LDS s16x4*)a; }
DI bf16x8 asb(u32x4 v) { return __builtin_bit_cast(bf16x8, v); }
DI s16x4 ass(u32x2 v) { return __builtin_bit_cast(s16x4, v); }
DI void st8_sc1(void* p, u32x2 v) { asm volatile("global_store_dwordx2 %0, %1, off sc1" :: "v"(p), "v"(v) : "memory"); }
DI void flag_set(char* ws, int idx, unsigned long long tag) { __hip_atomic_store((gu64*)(ws + WS_FLAG) + idx, tag, RLX_AGENT); }
DI void flag_wait(char* ws, int idx0, int idx1, unsigned long long tag) {
  gu64* f = (gu64*)(ws + WS_FLAG);
  for (unsigned spins = 0; spins < 2000000u; ++spins) {
    bool ok = true;
    if (idx0 >= 0) ok = __hip_atomic_load(f + idx0, RLX_AGENT) == tag;
    if (idx1 >= 0) ok = ok && __hip_atomic_load(f + idx1, RLX_AGENT) == tag;
    if (__all(ok)) break;
    __builtin_amdgcn_s_sleep(1);
  }
}
DI void flag_wait3(char* ws, int idx0, int idx1, int idx2, unsigned long long tag) {
  gu64* f = (gu64*)(ws + WS_FLAG);
  for (unsigned spins = 0; spins < 2000000u; ++spins) {
    const unsigned long long a = __hip_atomic_load(f + idx0, RLX_AGENT), b = __hip_atomic_load(f + idx1, RLX_AGENT), c = __hip_atomic_load(f + idx2, RLX_AGENT);
    if (__all(a == tag && b == tag && c == tag)) break;
    __builtin_amdgcn_s_sleep(1);
  }
}
DI void acquire_agent() { __builtin_amdgcn_fence(__ATOMIC_ACQUIRE, "agent"); asm volatile("s_waitcnt vmcnt(0)" ::: "memory"); }
DI void st16_sc1(void* p, u32x4 v) { asm volatile("global_store_dwordx4 %0, %1, off sc1\n\ts_nop 1" :: "v"(p), "v"(v) : "memory"); }
struct Stamps {};
#define STAMP(i) do {} while (0)
#define RTSTAMP(i) do {} while (0)
DI void stamps_init(Stamps&) {}
DI void stamps_flush(Stamps&, char*) {}

constexpr int P_XROW = 1296;
constexpr int P_W = 0;
constexpr int P_X = 51200;
constexpr int P_KV = P_X + 16 * P_XROW;
constexpr int P_P = P_KV + 640;
static_assert(P_P + 9216 <= 81920, "lds map");

template <int MODE> DI void retile(const char* src  , char* dst  , unsigned lds, int lane) {
  constexpr int ROWS = MODE ? 80 : 64, NP = (ROWS * 10 + 63) / 64;
#pragma unroll
  for (int i = 0; i < NP; ++i) {
    const int ci = 64 * i + lane, r = ci / 10;
    glds_m0(lds + i * 1024);
    if (ci < ROWS * 10) glds_go<0>(src, r * 1280 + (ci - 10 * r) * 16);
  }
  WAITV(0);
  const int g4 = lane >> 4, qq = (lane & 15) >> 2, pp = lane & 3, hf = g4 >> 1;
  const unsigned col = (16 * (g4 & 1) + 4 * pp) * 2;
#pragma unroll
  for (int u = 0; u < (MODE ? 5 : 4); ++u) {
    int r0, r1;
    if (MODE == 0) { r0 = 16 * u + 8 * hf + qq; r1 = r0 + 4; }
    else if (u < 4) { r0 = 20 * u + 4 * hf + qq; r1 = r0 + 8; }
    else { r0 = 20 * (2 * hf) + 16 + qq; r1 = r0 + 20; }
    const unsigned a0 = lds + r0 * 160 + col, a1 = lds + r1 * 160 + col;
    const bf16x8 t0 = cat8(trread(a0), trread(a1)), t1 = cat8(trread(a0 + 64), trread(a1 + 64)), t2 = cat8(trread(a0 + 128), trread(a1 + 128));
    char* d = dst + u * 2560;
    st16_sc1(d + lane * 16, __builtin_bit_cast(u32x4, t0));
    st16_sc1(d + 1024 + lane * 16, __builtin_bit_cast(u32x4, t1));
    if ((lane & 31) < 16) st16_sc1(d + 2048 + ((lane >> 5) * 16 + (lane & 15)) * 16, __builtin_bit_cast(u32x4, t2));
  }
  WAITV(0);
}
DI int retile_unit(int unit, const char* wq, const char* wo, const char* wd, char* ws, unsigned lds, int lane) {
  if (unit < 80) { const int g = unit & 7, kt = unit >> 3; retile<0>(wq + (size_t)kt * 64 * 1280 + g * 160, ws + WS_WQ + (size_t)(g * 40 + kt * 4) * 2560, lds, lane); return FL_WQ + g * 16 + kt; }
  if (unit < 144) { const int v = unit - 80, g = v & 7, og = v >> 3; retile<1>(wo + (size_t)og * 80 * 1280 + g * 160, ws + WS_WO + (size_t)(g * 40 + og * 5) * 2560, lds, lane); return FL_WO + g * 8 + og; }
  if (unit < 336) { const int v = unit - 144, g = v & 7, kt = v >> 3; retile<0>(wd + (size_t)kt * 64 * 1280 + g * 160, ws + WS_WD + (size_t)(g * 96 + kt * 4) * 2560, lds, lane); return FL_WD + g * 32 + kt; }
  return -1;
}

DI float row16_sum(float v) {
  v += __builtin_bit_cast(float, __builtin_amdgcn_update_dpp(0, __builtin_bit_cast(int, v), 0xB1, 0xf, 0xf, true));
  v += __builtin_bit_cast(float, __builtin_amdgcn_update_dpp(0, __builtin_bit_cast(int, v), 0x4E, 0xf, 0xf, true));
  v += __builtin_bit_cast(float, __builtin_amdgcn_update_dpp(0, __builtin_bit_cast(int, v), 0x141, 0xf, 0xf, true));
  v += __builtin_bit_cast(float, __builtin_amdgcn_update_dpp(0, __builtin_bit_cast(int, v), 0x140, 0xf, 0xf, true));
  return v;
}
DI u32x2 ln_kv_unit(int unit, const char* __restrict__ x, const char* __restrict__ wk, const char* __restrict__ wv, char* __restrict__ ws, unsigned sb, int w, int lane, Stamps& stp) {
  const int tid = w * 64 + lane;
  const int t0 = unit * 16;
  const int row = tid >> 4, c = tid & 15;
  const char* xr = x + (size_t)(t0 + row) * 1280 + c * 16;
  u32x4 v[5]; u32x2 ev;
  asm volatile("global_load_dwordx4 %0, %6, off\n\tglobal_load_dwordx4 %1, %6, off offset:256\n\tglobal_load_dwordx4 %2, %6, off offset:512\n\t"
               "global_load_dwordx4 %3, %6, off offset:768\n\tglobal_load_dwordx4 %4, %6, off offset:1024\n\tglobal_load_dwordx2 %5, %7, off sc1"
               : "=&v"(v[0]), "=&v"(v[1]), "=&v"(v[2]), "=&v"(v[3]), "=&v"(v[4]), "=&v"(ev) : "v"(xr), "v"(ws + WS_FLAG + FL_E * 8) : "memory");
#pragma unroll
  for (int i = 0; i < 12; ++i) { const int p = w + 4 * i; glds_m0(sb + P_W + p * 1024); glds_go<0>(p < 25 ? wk + p * 1024 : wv + (p - 25) * 1024, lane * 16); }
  if (w < 2) { glds_m0(sb + P_W + (48 + w) * 1024); glds_go<0>(wv + (23 + w) * 1024, lane * 16); }
  asm volatile("s_waitcnt vmcnt(13)" : "+v"(v[0]), "+v"(v[1]), "+v"(v[2]), "+v"(v[3]), "+v"(v[4]), "+v"(ev) :: "memory");
  if (w >= 2) asm volatile("s_waitcnt vmcnt(12)" : "+v"(v[0]), "+v"(v[1]), "+v"(v[2]), "+v"(v[3]), "+v"(v[4]), "+v"(ev) :: "memory");
  RTSTAMP(4);
  float s = 0.f;
#pragma unroll
  for (int i = 0; i < 5; ++i)
#pragma unroll
    for (int e = 0; e < 4; ++e) s += bflo(v[i][e]) + bfhi(v[i][e]);
  s = row16_sum(s);
  const float mean = s * (1.f / D_);
  float q = 0.f;
#pragma unroll
  for (int i = 0; i < 5; ++i)
#pragma unroll
    for (int e = 0; e < 4; ++e) { float a = bflo(v[i][e]) - mean, b = bfhi(v[i][e]) - mean; q += a * a + b * b; }
  q = row16_sum(q);
  const float mb = rbf(mean), sd = rbf(__builtin_amdgcn_sqrtf(rbf(rbf(q * (1.f / D_)) + 1e-5f)));
  const float rs = __builtin_amdgcn_rcpf(sd);
  if (c == 0) st8_sc1(ws + WS_ST + (size_t)(t0 + row) * 8, u32x2{__float_as_uint(mb), __float_as_uint(rs)});
#pragma unroll
  for (int i = 0; i < 5; ++i) {
    u32x4 o;
#pragma unroll
    for (int e = 0; e < 4; ++e) o[e] = pk2(rbf(bflo(v[i][e]) - mb) * rs, rbf(bfhi(v[i][e]) - mb) * rs);
    *(LDS u32x4*)(sb + P_X + row * P_XROW + (c + 16 * i) * 16) = o;
  }
  RTSTAMP(5);
  WAITV(0);
  WAITL0(); BAR();
  {
    char* xnf = ws + WS_XN + (size_t)(t0 >> 5) * 40 * 1024 + ((t0 & 16) + (lane & 15)) * 16;
    u32x4 o[5];
#pragma unroll
    for (int i = 0; i < 5; ++i) { const int ch = 20 * w + 4 * i + (lane >> 4); o[i] = *(LDS u32x4*)(sb + P_X + (lane & 15) * P_XROW + ch * 16); }
#pragma unroll
    for (int i = 0; i < 5; ++i) { const int ch = 20 * w + 4 * i + (lane >> 4); st16_sc1(xnf + (ch >> 1) * 1024 + (ch & 1) * 512, o[i]); }
  }
  const int g4 = lane >> 4, i16 = lane & 15, qq = i16 >> 2, pp = i16 & 3;
  f32x4 a0 = {}, a1 = {}, a2 = {};
  {
    bf16x8 b[5], f0[5], f1[5], f2[5];
#pragma unroll
    for (int st = 0; st < 5; ++st) {
      const int k0 = 160 * w + 32 * st;
      b[st] = ldsr128(sb + P_X + i16 * P_XROW + (k0 + 8 * g4) * 2);
      const int kr = k0 + 8 * g4 + qq;
      const unsigned wkb = sb + P_W + kr * 40, wvb = wkb + 25600;
      const unsigned t2 = ((pp & 1) ? wvb : wkb) + 32;
      f0[st] = cat8(trread(wkb + 8 * pp), trread(wkb + 8 * pp + 160));
      f1[st] = cat8(trread(wvb + 8 * pp), trread(wvb + 8 * pp + 160));
      f2[st] = cat8(trread(t2), trread(t2 + 160));
    }
#pragma unroll
    for (int st = 0; st < 5; ++st) { a0 = MFMA16(f0[st], b[st], a0); a1 = MFMA16(f1[st], b[st], a1); a2 = MFMA16(f2[st], b[st], a2); }
  }
  RTSTAMP(6);
  const unsigned pb = sb + P_P + lane * 16;
  if (w != 0) *(LDS f32x4*)(pb + (0 * 3 + w - 1) * 1024) = a0;
  if (w != 1) *(LDS f32x4*)(pb + (1 * 3 + (w < 1 ? w : w - 1)) * 1024) = a1;
  if (w != 2) *(LDS f32x4*)(pb + (2 * 3 + (w < 2 ? w : w - 1)) * 1024) = a2;
  WAITL0(); BAR();
  char* img = ws + WS_KV + (size_t)(t0 >> 5) * KVB;
  const int half16 = (t0 >> 4) & 1, key = 16 * half16 + i16;
  if (w < 3) {
    const f32x4 own = w == 0 ? a0 : (w == 1 ? a1 : a2);
    const f32x4 s4 = own + *(LDS f32x4*)(pb + (w * 3 + 0) * 1024) + *(LDS f32x4*)(pb + (w * 3 + 1) * 1024) + *(LDS f32x4*)(pb + (w * 3 + 2) * 1024);
    const u32x2 pk = {pk2(s4[0], s4[1]), pk2(s4[2], s4[3])};
    if (w == 0) st8_sc1(img + (g4 & 1) * 512 + key * 16 + (g4 >> 1) * 8, pk);
    else if (w == 2 && g4 == 0) st8_sc1(img + KV_K1 + key * 8, pk);
    if (w == 1 || (w == 2 && g4 == 1)) {
      const int d0 = w == 1 ? 4 * g4 : 16;
      const unsigned va = sb + P_KV + d0 * 32 + ((i16 >> 2) & 1) * 16 + (4 * (i16 >> 3) + (i16 & 3)) * 2;
      *(LDS unsigned short*)(va) = (unsigned short)pk[0]; *(LDS unsigned short*)(va + 32) = (unsigned short)(pk[0] >> 16);
      *(LDS unsigned short*)(va + 64) = (unsigned short)pk[1]; *(LDS unsigned short*)(va + 96) = (unsigned short)(pk[1] >> 16);
    }
  } else if (lane < 4 && half16 == 0) {
    st16_sc1(img + KV_C + lane * 16, u32x4{lane == 0 ? 0x3f80u : 0u, 0u, 0u, 0u});
  }
  WAITL0(); BAR();
  if (w == 3) {
    const int hf = lane >> 5, r = lane & 31;
    if (r <= 20) {
      u32x4 o = {0x3f803f80u, 0x3f803f80u, 0x3f803f80u, 0x3f803f80u};
      if (r < 20) o = *(LDS u32x4*)(sb + P_KV + r * 32 + hf * 16);
      st16_sc1(img + KV_V + half16 * 672 + hf * 336 + r * 16, o);
    }
  }
  RTSTAMP(7);
  WAITV(0); BAR();
  return ev;
}

struct RFrag { bf16x8 a0, a1, a2, b0, b1; };
struct GAcc { f32x16 t[6]; };
typedef __amdgpu_buffer_rsrc_t rsrc_t;
DI rsrc_t mk_rsrc(const void* p) { return __builtin_amdgcn_make_buffer_rsrc((void*)p, 0, 0x7fffffff, 0x00020000); }
DI bf16x8 bload(rsrc_t r, unsigned voff, unsigned soff) { return __builtin_bit_cast(bf16x8, __builtin_amdgcn_raw_buffer_load_b128(r, voff, soff, 16)); }
template <int NU, int DEPTH, class HOOK> DI void rs_gemm(GAcc& c, const char* xt, unsigned xtile, const char* wg, int w, int lane, HOOK hook, Stamps& stp) {
  const unsigned l16 = lane * 16, l2 = 2048 + ((lane >> 5) * 16 + (lane & 15)) * 16;
  const rsrc_t rx = mk_rsrc(xt + w * 1024), rw = mk_rsrc(wg + w * 2560);
  RFrag f[DEPTH];
#pragma unroll
  for (int i = 0; i < DEPTH; ++i) if (i < NU) {
    f[i].a0 = bload(rw, l16, i * 10240); f[i].b0 = bload(rx, l16, i * 4096); f[i].b1 = bload(rx, l16, xtile + i * 4096);
    f[i].a1 = bload(rw, l16, i * 10240 + 1024); f[i].a2 = bload(rw, l2, i * 10240);
    SB0();
  }
  STAMP(11);
  hook();
  STAMP(12);
#pragma unroll
  for (int i = 0; i < NU; ++i) {
    RFrag& q = f[i % DEPTH];
    const bool ld = i + DEPTH < NU;
    const unsigned xo = (i + DEPTH) * 4096, wo = (i + DEPTH) * 10240;
    SB0(); c.t[0] = MFMA32(q.a0, q.b0, c.t[0]);
    SB0(); c.t[1] = MFMA32(q.a0, q.b1, c.t[1]);
    SB0(); if (ld) q.a0 = bload(rw, l16, wo);
    SB0(); c.t[2] = MFMA32(q.a1, q.b0, c.t[2]);
    SB0(); c.t[3] = MFMA32(q.a1, q.b1, c.t[3]);
    SB0(); if (ld) q.a1 = bload(rw, l16, wo + 1024);
    SB0(); c.t[4] = MFMA32(q.a2, q.b0, c.t[4]);
    SB0(); if (ld) q.b0 = bload(rx, l16, xo);
    SB0(); c.t[5] = MFMA32(q.a2, q.b1, c.t[5]);
    SB0(); if (ld) { q.b1 = bload(rx, l16, xtile + xo); q.a2 = bload(rw, l2, wo); }
    if (i == 0) STAMP(13);
    if (i == DEPTH - 1) STAMP(14);
    if (i == DEPTH) STAMP(15);
  }
  SB0();
}
DI void gemm_reduce(GAcc& c, f32x4 (&r)[6], unsigned xb, int w, int lane, Stamps& stp) {
  const unsigned l16 = lane * 16;
  f32x4 k0[6], k1[6];
  if (w < 2) {
#pragma unroll
    for (int t = 0; t < 6; ++t) {
      *(LDS f32x4*)(xb + ((w ^ 2) * 12 + 2 * t) * 1024 + l16) = f32x4{c.t[t][8], c.t[t][9], c.t[t][10], c.t[t][11]};
      *(LDS f32x4*)(xb + ((w ^ 2) * 12 + 2 * t + 1) * 1024 + l16) = f32x4{c.t[t][12], c.t[t][13], c.t[t][14], c.t[t][15]};
      k0[t] = f32x4{c.t[t][0], c.t[t][1], c.t[t][2], c.t[t][3]}; k1[t] = f32x4{c.t[t][4], c.t[t][5], c.t[t][6], c.t[t][7]};
    }
  } else {
#pragma unroll
    for (int t = 0; t < 6; ++t) {
      *(LDS f32x4*)(xb + ((w ^ 2) * 12 + 2 * t) * 1024 + l16) = f32x4{c.t[t][0], c.t[t][1], c.t[t][2], c.t[t][3]};
      *(LDS f32x4*)(xb + ((w ^ 2) * 12 + 2 * t + 1) * 1024 + l16) = f32x4{c.t[t][4], c.t[t][5], c.t[t][6], c.t[t][7]};
      k0[t] = f32x4{c.t[t][8], c.t[t][9], c.t[t][10], c.t[t][11]}; k1[t] = f32x4{c.t[t][12], c.t[t][13], c.t[t][14], c.t[t][15]};
    }
  }
  WAITL0(); BAR();
  STAMP(9);
#pragma unroll
  for (int t = 0; t < 6; ++t) { k0[t] += *(LDS f32x4*)(xb + (w * 12 + 2 * t) * 1024 + l16); k1[t] += *(LDS f32x4*)(xb + (w * 12 + 2 * t + 1) * 1024 + l16); }
  WAITL0(); BAR();
  STAMP(10);
#pragma unroll
  for (int t = 0; t < 6; ++t) {
    if (w & 1) { *(LDS f32x4*)(xb + ((w ^ 1) * 6 + t) * 1024 + l16) = k0[t]; r[t] = k1[t]; }
    else { *(LDS f32x4*)(xb + ((w ^ 1) * 6 + t) * 1024 + l16) = k1[t]; r[t] = k0[t]; }
  }
  WAITL0(); BAR();
  STAMP(11);
#pragma unroll
  for (int t = 0; t < 6; ++t) r[t] += *(LDS f32x4*)(xb + (w * 6 + t) * 1024 + l16);
}

DI void gemm_reduce1(GAcc& c, f32x4 (&r)[6], unsigned xb, int w, int lane) {
  const unsigned l16 = lane * 16;
#pragma unroll
  for (int d = 0; d < 4; ++d) {
    if (d != w) {
      const unsigned o = xb + (d * 3 + ((w - d - 1) & 3)) * 6144 + l16;
#pragma unroll
      for (int t = 0; t < 6; ++t) *(LDS f32x4*)(o + t * 1024) = f32x4{c.t[t][4 * d], c.t[t][4 * d + 1], c.t[t][4 * d + 2], c.t[t][4 * d + 3]};
    } else {
#pragma unroll
      for (int t = 0; t < 6; ++t) r[t] = f32x4{c.t[t][4 * d], c.t[t][4 * d + 1], c.t[t][4 * d + 2], c.t[t][4 * d + 3]};
    }
  }
  WAITL0(); BAR();
#pragma unroll
  for (int d = 0; d < 3; ++d)
#pragma unroll
    for (int t = 0; t < 6; ++t) r[t] += *(LDS f32x4*)(xb + ((w * 3 + d) * 6 + t) * 1024 + l16);
}

constexpr int A_SLOT = 8 * KVB;
constexpr int A_QI = 61440;
constexpr int A_MINI = A_QI + 64 * 176;
constexpr float QSCALE = 0.22360679774997896f * 1.4426950408889634f;
constexpr float MDELTA = 90.f;
static_assert(A_MINI + 8192 <= 81920 && 3 * A_SLOT <= 81920, "lds map");

DI void kv_issue(const char* kvimg, int chunk, unsigned dst, unsigned l16, int w) {
  const char* src = kvimg + (size_t)chunk * A_SLOT + w * 5120;
  glds_m0(dst + w * 5120);
  glds_go_sc1<0>(src, l16); glds_go_sc1<1024>(src, l16); glds_go_sc1<2048>(src, l16); glds_go_sc1<3072>(src, l16);
  glds_m0(dst + w * 5120 + 4096);
  glds_go_sc1<0>(src + 4096, l16);
  if (w == 3) glds_go_sc1<1024>(src + 4096, l16);
}

DI void attn_slow(const char* kvimg, u32x4 q0, u32x2 q1, int lane, f32x16& O) {
  const int hf = lane >> 5, r = lane & 31;
  if (hf) { q1[0] = 0u; q1[1] = 0u; }
  const unsigned ov = KV_V + hf * 336 + (r < 20 ? r : 20) * 16, o1 = hf ? KV_C : KV_K1 + r * 8;
  const rsrc_t rk = mk_rsrc(kvimg);
  float m = -3.0e38f;
  f32x16 z = {};
  O = z;
  for (int kb = 0; kb < 64; ++kb) {
    const unsigned bo = kb * KVB;
    const u32x4 k0 = __builtin_amdgcn_raw_buffer_load_b128(rk, lane * 16, bo, 16);
    const u32x2 k1 = __builtin_amdgcn_raw_buffer_load_b64(rk, o1, bo, 16);
    const u32x4 v0 = __builtin_amdgcn_raw_buffer_load_b128(rk, ov, bo, 16), v1 = __builtin_amdgcn_raw_buffer_load_b128(rk, ov + 672, bo, 16);
    f32x16 s = MFMA32(asb(k0), asb(q0), z);
    s = MFMA32K8(ass(k1), ass(q1), s);
    float t = s[0];
#pragma unroll
    for (int i = 1; i < 16; ++i) t = fmaxf(t, s[i]);
    t = fmaxf(t, __shfl_xor(t, 32));
    const float mn = fmaxf(m, t), al = __builtin_amdgcn_exp2f(m - mn);
    m = mn;
    u32x4 p0, p1;
#pragma unroll
    for (int i = 0; i < 4; ++i) { p0[i] = pk2(__builtin_amdgcn_exp2f(s[2 * i] - mn), __builtin_amdgcn_exp2f(s[2 * i + 1] - mn));
                                  p1[i] = pk2(__builtin_amdgcn_exp2f(s[8 + 2 * i] - mn), __builtin_amdgcn_exp2f(s[8 + 2 * i + 1] - mn)); }
#pragma unroll
    for (int i = 0; i < 16; ++i) O[i] *= al;
    O = MFMA32(asb(v0), asb(p0), O);
    O = MFMA32(asb(v1), asb(p1), O);
  }
}

DI void attn_store(const f32x16& O, char* of  , int w, int lane) {
  const int hf = lane >> 5, r = lane & 31;
  const float l = __shfl(O[8], r + 32);
  const float s = 1.f / l;
  st16_sc1(of + w * 1024 + lane * 16, u32x4{pk2(O[0] * s, O[1] * s), pk2(O[2] * s, O[3] * s), pk2(O[4] * s, O[5] * s), pk2(O[6] * s, O[7] * s)});
  if (!hf) st8_sc1(of + 4096 + ((w >> 1) * 32 + r) * 16 + (w & 1) * 8, u32x2{pk2(O[8] * s, O[9] * s), pk2(O[10] * s, O[11] * s)});
}

DI void attn_role(char* __restrict__ ws, int xcd, int j, unsigned sb, int w, int lane, unsigned long long tag, Stamps& stp) {
  const int r32 = lane & 31, hf = lane >> 5;
  const int rb = j >> 3, g = j & 7, T0 = 256 * xcd + 64 * rb;
  const char* kvimg = ws + WS_KV;
  const unsigned l16 = lane * 16;
  if (w == 0) flag_wait(ws, lane < 10 ? FL_WQ + g * 16 + lane : (lane < 14 ? FL_XN + (T0 >> 4) + lane - 10 : -1), -1, tag);
  BAR();
  RTSTAMP(2);
  STAMP(0);
#define APRIO0 0
  __builtin_amdgcn_s_setprio(APRIO0);
  u32x4 qA0, qB0; u32x2 qA1, qB1;
  {
    GAcc c;
#pragma unroll
    for (int t = 0; t < 6; ++t) c.t[t] = f32x16{};
    u32x2 fa, fb;
    rs_gemm<10, 5>(c, ws + WS_XN + (size_t)(T0 >> 5) * 40 * 1024, 40 * 1024, ws + WS_WQ + (size_t)g * 40 * 2560, w, lane, [&] {
      const char* fp = ws + WS_FLAG + (FL_XN + lane) * 8;
      asm volatile("global_load_dwordx2 %0, %2, off sc1\n\tglobal_load_dwordx2 %1, %2, off offset:512 sc1" : "=&v"(fa), "=&v"(fb) : "v"(fp) : "memory"); }, stp);
    asm volatile("s_waitcnt vmcnt(0)" : "+v"(fa), "+v"(fb) :: "memory");
    if (w == 0) {
      const unsigned tl = (unsigned)tag, th = (unsigned)(tag >> 32);
      if (!__all(fa[0] == tl && fa[1] == th && fb[0] == tl && fb[1] == th)) flag_wait(ws, FL_XN + lane, FL_XN + 64 + lane, tag);
    }
    STAMP(5);
    f32x4 r[6];
#pragma unroll
    for (int d = 0; d < 4; ++d) {
      if (d != w) {
        const int si = (w - d - 1) & 3;
#pragma unroll
        for (int t = 0; t < 4; ++t) *(LDS f32x4*)(sb + ((d * 3 + si) * 4 + t) * 1024 + l16) = f32x4{c.t[t][4 * d], c.t[t][4 * d + 1], c.t[t][4 * d + 2], c.t[t][4 * d + 3]};
        if (d < 2) {
#pragma unroll
          for (int t = 4; t < 6; ++t) *(LDS f32x4*)(sb + 49152 + ((d * 3 + si) * 2 + t - 4) * 1024 + l16) = f32x4{c.t[t][4 * d], c.t[t][4 * d + 1], c.t[t][4 * d + 2], c.t[t][4 * d + 3]};
        }
      } else {
#pragma unroll
        for (int t = 0; t < 6; ++t) r[t] = f32x4{c.t[t][4 * d], c.t[t][4 * d + 1], c.t[t][4 * d + 2], c.t[t][4 * d + 3]};
      }
    }
    WAITV(0); WAITL0(); BAR();
    STAMP(6);
    glds_m0(sb + A_MINI + w * 2048); glds_go_sc1<0>(kvimg + w * 2048, l16); glds_go_sc1<1024>(kvimg + w * 2048, l16);
#pragma unroll
    for (int s3 = 0; s3 < 3; ++s3) {
#pragma unroll
      for (int t = 0; t < 4; ++t) r[t] += *(LDS f32x4*)(sb + ((w * 3 + s3) * 4 + t) * 1024 + l16);
      if (w < 2) {
#pragma unroll
        for (int t = 4; t < 6; ++t) r[t] += *(LDS f32x4*)(sb + 49152 + ((w * 3 + s3) * 2 + t - 4) * 1024 + l16);
      }
    }
    STAMP(7);
#pragma unroll
    for (int a = 0; a < 3; ++a) {
      if (a == 2 && w >= 2) continue;
#pragma unroll
      for (int tt = 0; tt < 2; ++tt) {
        const f32x4 v = r[2 * a + tt];
        *(LDS u32x2*)(sb + A_QI + (32 * tt + r32) * 176 + (32 * a + 8 * w + 4 * hf) * 2) = u32x2{pk2(rbf(v[0]) * QSCALE, rbf(v[1]) * QSCALE), pk2(rbf(v[2]) * QSCALE, rbf(v[3]) * QSCALE)};
      }
    }
    WAITV(0); WAITL0(); BAR();
    if (w == 0 && lane == 0) flag_set(ws, FL_QD + xcd + 8 * j, tag);
    STAMP(8);
    kv_issue(kvimg, 0, sb, l16, w);
    kv_issue(kvimg, 1, sb + A_SLOT, l16, w);
    const unsigned qi = sb + A_QI + r32 * 176 + 40 * w + 8 * hf;
    const u32x2 a0 = *(LDS u32x2*)qi, a1 = *(LDS u32x2*)(qi + 16), b0 = *(LDS u32x2*)(qi + 32 * 176), b1 = *(LDS u32x2*)(qi + 32 * 176 + 16);
    const u32x2 a2 = *(LDS u32x2*)(qi - 8 * hf + 32), b2 = *(LDS u32x2*)(qi - 8 * hf + 32 * 176 + 32);
    qA0 = u32x4{a0[0], a0[1], a1[0], a1[1]}; qB0 = u32x4{b0[0], b0[1], b1[0], b1[1]};
    qA1 = hf ? u32x2{0u, 0u} : a2; qB1 = hf ? u32x2{0u, 0u} : b2;
  }
  STAMP(1);
  const unsigned oK = lane * 16, oV = KV_V + hf * 336 + (r32 < 20 ? r32 : 20) * 16, o1 = hf ? KV_C : KV_K1 + r32 * 8;
  const f32x16 Z = {};
  {
    float mA = -3.0e38f, mB = -3.0e38f;
#pragma unroll
    for (int b = 0; b < 3; ++b) {
      const bf16x8 k0 = ldsr128(sb + A_MINI + b * KVB + oK); const s16x4 k1 = ldsr64(sb + A_MINI + b * KVB + o1);
      f32x16 sa = MFMA32K8(k1, ass(qA1), MFMA32(k0, asb(qA0), Z));
      f32x16 sbb = MFMA32K8(k1, ass(qB1), MFMA32(k0, asb(qB0), Z));
#pragma unroll
      for (int i = 0; i < 16; i += 2) { mA = __builtin_fmaxf(__builtin_fmaxf(mA, sa[i]), sa[i + 1]); mB = __builtin_fmaxf(__builtin_fmaxf(mB, sbb[i]), sbb[i + 1]); }
    }
    mA = fmaxf(mA, __shfl_xor(mA, 32)); mB = fmaxf(mB, __shfl_xor(mB, 32));
    if (hf) { qA1[0] = pk2(-(mA + MDELTA), 0.f); qB1[0] = pk2(-(mB + MDELTA), 0.f); }
  }
  if (w == 3) WAITV(6); else WAITV(5);
  BAR();
  __builtin_amdgcn_s_setprio(0);
  STAMP(2);
  f32x16 OA = Z, OB = Z, SA, SB;
  u32x4 pA0, pA1, pB0, pB1;
  f32x4 e0, e1;
  bf16x8 k0a, k0b, v0a, v0b, v1a, v1b; s16x4 k1a, k1b;
#define EXP4(e, s, g) do { e[0] = __builtin_amdgcn_exp2f(s[4 * (g)]); e[1] = __builtin_amdgcn_exp2f(s[4 * (g) + 1]); e[2] = __builtin_amdgcn_exp2f(s[4 * (g) + 2]); e[3] = __builtin_amdgcn_exp2f(s[4 * (g) + 3]); } while (0)
#define CVT(p, h, e) do { p[2 * (h)] = pk2(e[0], e[1]); p[2 * (h) + 1] = pk2(e[2], e[3]); } while (0)
  unsigned cK = sb + oK, cV = sb + oV, c1 = sb + o1;
  k0a = ldsr128(cK); k1a = ldsr64(c1);
  k0b = ldsr128(cK + KVB); k1b = ldsr64(c1 + KVB);
  v0a = ldsr128(cV); v1a = ldsr128(cV + 672);
  SA = MFMA32K8(k1a, ass(qA1), MFMA32(k0a, asb(qA0), Z));
  SB = MFMA32K8(k1a, ass(qB1), MFMA32(k0a, asb(qB0), Z));
  EXP4(e0, SA, 0); CVT(pA0, 0, e0); EXP4(e0, SA, 1); CVT(pA0, 1, e0);
  EXP4(e0, SA, 2); CVT(pA1, 0, e0); EXP4(e1, SA, 3);
#define PERIOD(KC0, KC1, VC0, VC1, KN0, KN1, VN0, VN1, aV, aK, a1, LOADN) do {                           \
    SB0(); OA = MFMA32(VC0, asb(pA0), OA);     SB0(); EXP4(e0, SB, 0); SB0(); CVT(pA1, 1, e1); SB0();      \
    SA = MFMA32(KC0, asb(qA0), Z);             SB0(); EXP4(e1, SB, 1); SB0(); CVT(pB0, 0, e0); SB0();      \
    SA = MFMA32K8(KC1, ass(qA1), SA);          SB0();                                                      \
    VN0 = ldsr128(aV); VN1 = ldsr128((aV) + 672);                                                          \
    if (LOADN) { KN0 = ldsr128(aK); KN1 = ldsr64(a1); }                                                    \
    SB0(); EXP4(e0, SB, 2); SB0(); CVT(pB0, 1, e1); SB0();                                                 \
    OA = MFMA32(VC1, asb(pA1), OA);            SB0(); EXP4(e1, SB, 3); SB0(); CVT(pB1, 0, e0); SB0();      \
    OB = MFMA32(VC0, asb(pB0), OB);            SB0(); EXP4(e0, SA, 0); SB0(); CVT(pB1, 1, e1); SB0();      \
    SB = MFMA32(KC0, asb(qB0), Z);             SB0(); EXP4(e1, SA, 1); SB0(); CVT(pA0, 0, e0); SB0();      \
    SB = MFMA32K8(KC1, ass(qB1), SB);          SB0(); EXP4(e0, SA, 2); SB0(); CVT(pA0, 1, e1); SB0();      \
    WAITL0();                                                                                              \
    OB = MFMA32(VC1, asb(pB1), OB);            SB0(); EXP4(e1, SA, 3); SB0(); CVT(pA1, 0, e0); SB0();      \
  } while (0)
#define PER_ODD(pV, bV, pK, p1, bK, L)  PERIOD(k0b, k1b, v0a, v1a, k0a, k1a, v0b, v1b, (pV) + (bV) * KVB, (pK) + (bK) * KVB, (p1) + (bK) * KVB, L)
#define PER_EVEN(pV, bV, pK, p1, bK, L) PERIOD(k0a, k1a, v0b, v1b, k0b, k1b, v0a, v1a, (pV) + (bV) * KVB, (pK) + (bK) * KVB, (p1) + (bK) * KVB, L)
  PER_ODD(cV, 1, cK, c1, 2, true); PER_EVEN(cV, 2, cK, c1, 3, true); PER_ODD(cV, 3, cK, c1, 4, true);
  PER_EVEN(cV, 4, cK, c1, 5, true); PER_ODD(cV, 5, cK, c1, 6, true); PER_EVEN(cV, 6, cK, c1, 7, true);
  int slot = 0;
  for (int c = 0; c < 7; ++c) {
    const int ns = slot == 2 ? 0 : slot + 1;
    const unsigned nb = sb + ns * A_SLOT;
    unsigned nK = nb + oK, nV = nb + oV, n1 = nb + o1;
    asm volatile("" : "+v"(nK), "+v"(nV), "+v"(n1));
    WAITV(0); BAR();
    if (c + 2 < 8) {
      const int fs = ns == 2 ? 0 : ns + 1;
      __builtin_amdgcn_s_setprio(3);
      kv_issue(kvimg, c + 2, sb + fs * A_SLOT, l16, w);
      __builtin_amdgcn_s_setprio(0);
    }
    PER_ODD(cV, 7, nK, n1, 0, true);
    PER_EVEN(nV, 0, nK, n1, 1, true); PER_ODD(nV, 1, nK, n1, 2, true); PER_EVEN(nV, 2, nK, n1, 3, true); PER_ODD(nV, 3, nK, n1, 4, true);
    PER_EVEN(nV, 4, nK, n1, 5, true); PER_ODD(nV, 5, nK, n1, 6, true); PER_EVEN(nV, 6, nK, n1, 7, true);
    cK = nK; cV = nV; c1 = n1; slot = ns;
  }
  PER_ODD(cV, 7, cK, c1, 0, false);
  CVT(pA1, 1, e1);
  EXP4(e0, SB, 0); CVT(pB0, 0, e0); EXP4(e0, SB, 1); CVT(pB0, 1, e0);
  OA = MFMA32(v0b, asb(pA0), OA);
  OA = MFMA32(v1b, asb(pA1), OA);
  OB = MFMA32(v0b, asb(pB0), OB);
  EXP4(e0, SB, 2); CVT(pB1, 0, e0); EXP4(e0, SB, 3); CVT(pB1, 1, e0);
  OB = MFMA32(v1b, asb(pB1), OB);
#undef PERIOD
  STAMP(3);
  __builtin_amdgcn_s_setprio(3);
  {
    bool okA = true, okB = true;
#pragma unroll
    for (int i = 0; i < 12; ++i) { okA = okA && (fabsf(OA[i]) < 1.0e36f); okB = okB && (fabsf(OB[i]) < 1.0e36f); }
    if (__any(!okA)) attn_slow(kvimg, qA0, qA1, lane, OA);
    if (__any(!okB)) attn_slow(kvimg, qB0, qB1, lane, OB);
  }
  char* of = ws + WS_O + ((size_t)(T0 >> 5) * 40 + 5 * g) * 1024;
  attn_store(OA, of, w, lane);
  attn_store(OB, of + 40 * 1024, w, lane);
  WAITV(0);
  if (lane == 0) flag_set(ws, FL_O + (T0 >> 6) * 32 + g * 4 + w, tag);
  STAMP(4);
}

constexpr int F1_W = 12288;
constexpr int F1_X = 4 * F1_W;
#define GPRIO 0
#define GVPRIO 1
#define G_STOP 0
#define HT 0
#define F1_PART 1
DI void ffn1_map(int xcd, int j, int& T1, int& hc) {
  T1 = 1024 * (xcd >> 2) + 128 * (j >> 2); hc = 384 * (xcd & 3) + 96 * (j & 3);
}
struct F1Frag { bf16x8 a0, a1, a2, b0, b1; };
DI float silu_mul(float g, float u) { return g * u * __builtin_amdgcn_rcpf(1.f + __builtin_amdgcn_exp2f(g * -1.4426950408889634f)); }

DI void ffn_role(const char* __restrict__ x, const char* __restrict__ wg, const char* __restrict__ wu, char* __restrict__ ws, char* __restrict__ out, int xcd, int j, unsigned sb, int w, int lane, Stamps& stp) {
  unsigned long long tag;
  const int hf = lane >> 5, r32 = lane & 31, g4 = lane >> 4, qq = (lane & 15) >> 2, pp = lane & 3;
  const unsigned l16 = lane * 16;
  int T1, hc; ffn1_map(xcd, j, T1, hc);
  {
    const int wr = w >> 1, wc = w & 1;
    f32x16 c[6], cs[3];
#pragma unroll
    for (int t = 0; t < 6; ++t) c[t] = f32x16{};
#pragma unroll
    for (int t = 0; t < 3; ++t) cs[t] = f32x16{};
    u32x4 ones_u = {0x3f803f80u, 0x3f803f80u, 0x3f803f80u, 0x3f803f80u};
    asm volatile("" : "+v"(ones_u));
    const bf16x8 ones = __builtin_bit_cast(bf16x8, ones_u);
    const int krow = 8 * (g4 >> 1) + qq;
    const unsigned inpair = (pp & 1) * 16 + (g4 & 1) * 8;
    const int t0 = 3 * wr;
    unsigned ag0, ag1, ag2;
    if (pp >> 1) { ag0 = 6144 + krow * 192 + 32 * (t0 ^ 1) + inpair; ag1 = 6144 + krow * 192 + 32 * ((t0 + 1) ^ 1) + inpair; ag2 = 6144 + krow * 192 + 32 * ((t0 + 2) ^ 1) + inpair; }
    else { ag0 = krow * 192 + 32 * t0 + inpair; ag1 = ag0 + 32; ag2 = ag0 + 64; }
    ag0 += sb; ag1 += sb; ag2 += sb;
    const unsigned bsw = (r32 >> 1) & 7, brow = sb + F1_X + (64 * wc + r32) * 128;
    const unsigned bx0 = brow + ((hf ^ bsw) << 4), bx1 = brow + (((2 + hf) ^ bsw) << 4), bx2 = brow + (((4 + hf) ^ bsw) << 4), bx3 = brow + (((6 + hf) ^ bsw) << 4);
    unsigned ow[3];
#pragma unroll
    for (int i = 0; i < 3; ++i) { const int ci = 64 * (3 * (w & 1) + i) + lane, k = ci / 12, cc = ci - 12 * k; ow[i] = k * 3072 + ((w >> 1) ? (cc ^ 2) : cc) * 16 - i * 1024; }
    const char* wb = ((w >> 1) ? wu : wg) + hc * 2;
    unsigned ox[4];
#pragma unroll
    for (int i = 0; i < 4; ++i) ox[i] = (8 * i + (lane >> 3)) * 1280 + (((lane & 7) ^ ((4 * i + (lane >> 4)) & 7)) << 4) - i * 1024;
    const char* xb = x + (size_t)(T1 + 32 * w) * 1280;
    const unsigned wdst = sb + (w >> 1) * 6144 + (w & 1) * 3072, xdst = sb + F1_X + w * 4096;
    auto ld_w = [&](int s, int i) {
      if (i == 0) { glds_m0(wdst + (s & 3) * F1_W); glds_go<0>(wb + (size_t)s * 32 * 3072, ow[0]); }
      else if (i == 1) glds_go<1024>(wb + (size_t)s * 32 * 3072, ow[1]);
      else glds_go<2048>(wb + (size_t)s * 32 * 3072, ow[2]);
    };
    auto ld_x = [&](int u, int i) {
      if (i == 0) { glds_m0(xdst + (u & 1) * 16384); glds_go<0>(xb + u * 128, ox[0]); }
      else if (i == 1) glds_go<1024>(xb + u * 128, ox[1]);
      else if (i == 2) glds_go<2048>(xb + u * 128, ox[2]);
      else glds_go<3072>(xb + u * 128, ox[3]);
    };
    auto issue_w = [&](int s) { ld_w(s, 0); ld_w(s, 1); ld_w(s, 2); };
    auto issue_x = [&](int u) { ld_x(u, 0); ld_x(u, 1); ld_x(u, 2); ld_x(u, 3); };
    auto step = [&](auto Qc, auto Uc, auto RDc, auto LXc, auto LWc, const F1Frag& cur, F1Frag& nxt, int lx, int lw) {
      constexpr int Q = decltype(Qc)::value, U = decltype(Uc)::value; constexpr bool RD = decltype(RDc)::value, LX = decltype(LXc)::value, LW = decltype(LWc)::value;
      constexpr unsigned so = (Q & 3) * F1_W + U * 16 * 192;
      constexpr unsigned cbo = ((Q >> 1) & 1) * 16384;
      constexpr int US = 2 * (Q & 1) + U;
      const unsigned bb = (US == 0 ? bx0 : US == 1 ? bx1 : US == 2 ? bx2 : bx3);
      const bool mine = (1 - U) == wc;
      SB0(); c[0] = MFMA32(cur.a0, cur.b0, c[0]); SB0();
      if constexpr (RD) nxt.a0 = cat8(trread(ag0 + so), trread(ag0 + so + 768));
      if constexpr (LX) ld_x(lx, 0); else if constexpr (LW) ld_w(lw, 0);
      SB0(); c[1] = MFMA32(cur.a0, cur.b1, c[1]); SB0();
      if (mine) cs[0] = MFMA32(cur.a0, ones, cs[0]);
      SB0();
      if constexpr (RD) nxt.b0 = ldsr128(bb + cbo);
      if constexpr (LX) ld_x(lx, 1); else if constexpr (LW) ld_w(lw, 1);
      SB0(); c[2] = MFMA32(cur.a1, cur.b0, c[2]); SB0();
      if constexpr (RD) nxt.b1 = ldsr128(bb + cbo + 4096);
      if constexpr (LX) ld_x(lx, 2); else if constexpr (LW) ld_w(lw, 2);
      SB0(); c[3] = MFMA32(cur.a1, cur.b1, c[3]); SB0();
      if (mine) cs[1] = MFMA32(cur.a1, ones, cs[1]);
      SB0();
      if constexpr (RD) nxt.a1 = cat8(trread(ag1 + so), trread(ag1 + so + 768));
      if constexpr (LX) ld_x(lx, 3);
      SB0(); c[4] = MFMA32(cur.a2, cur.b0, c[4]); SB0();
      if constexpr (RD) nxt.a2 = cat8(trread(ag2 + so), trread(ag2 + so + 768));
      SB0(); c[5] = MFMA32(cur.a2, cur.b1, c[5]); SB0();
      if (mine) cs[2] = MFMA32(cur.a2, ones, cs[2]);
      SB0();
    };
#define IC(v) std::integral_constant<int, (v)>{}
#define BC(v) std::integral_constant<bool, (v)>{}
#define SYNC(n) do { WAITV(n); WAITL0(); BAR(); } while (0)
    STAMP(0);
    u32x2 ev;
    asm volatile("global_load_dwordx2 %0, %1, off sc1" : "=&v"(ev) : "v"(ws + WS_FLAG + FL_E * 8) : "memory");
    issue_x(0); issue_w(0); issue_w(1); issue_x(1); issue_w(2);
    F1Frag f0, f1;
    asm volatile("s_waitcnt vmcnt(10)" : "+v"(ev) :: "memory");
    tag = (((unsigned long long)__builtin_amdgcn_readfirstlane(ev[1]) << 32) | __builtin_amdgcn_readfirstlane(ev[0])) + 1ull;
    asm volatile("" :: "s"(tag));
    SYNC(10);
    if (w == 0 && lane == 0) flag_set(ws, FL_S + blockIdx.x, tag);
    issue_w(3);
    f0.a0 = cat8(trread(ag0), trread(ag0 + 768)); f0.a1 = cat8(trread(ag1), trread(ag1 + 768)); f0.a2 = cat8(trread(ag2), trread(ag2 + 768));
    f0.b0 = ldsr128(bx0); f0.b1 = ldsr128(bx0 + 4096);
    auto quad = [&](auto FIRSTc, auto LASTc, int s) {
      constexpr bool FIRST = decltype(FIRSTc)::value, LAST = decltype(LASTc)::value;
      step(IC(0), IC(1), BC(true), BC(false), BC(!FIRST), f0, f1, 0, s + 3);
      SYNC(10);
      step(IC(1), IC(0), BC(true), BC(false), BC(!LAST), f1, f0, 0, s + 4);
      step(IC(1), IC(1), BC(true), BC(false), BC(false), f0, f1, 0, 0);
      if constexpr (LAST) SYNC(3); else SYNC(6);
      step(IC(2), IC(0), BC(true), BC(!LAST), BC(false), f1, f0, s / 2 + 2, 0);
      step(IC(2), IC(1), BC(true), BC(false), BC(!LAST), f0, f1, 0, s + 5);
      if constexpr (LAST) SYNC(0); else SYNC(10);
      step(IC(3), IC(0), BC(true), BC(false), BC(!LAST), f1, f0, 0, s + 6);
      step(IC(3), IC(1), BC(true), BC(false), BC(false), f0, f1, 0, 0);
      if constexpr (!LAST) {
        SYNC(6);
        step(IC(4), IC(0), BC(true), BC(true), BC(false), f1, f0, s / 2 + 3, 0);
      } else step(IC(0), IC(0), BC(false), BC(false), BC(false), f1, f0, 0, 0);
    };
    STAMP(8);
    quad(BC(true), BC(false), 0);
    STAMP(9);
#pragma unroll 1
    for (int s = 4; s < 16; s += 4) {
      if (s == QD_AT) flag_wait(ws, lane == 0 ? FL_QD + xcd + 8 * j : -1, -1, tag);
      quad(BC(false), BC(false), s);
    }
    STAMP(10);
    flag_wait(ws, lane < 8 ? FL_XN + (T1 >> 4) + lane : -1, -1, tag);
    CFENCE();
    const rsrc_t rst = mk_rsrc(ws + WS_ST + (size_t)(T1 + 64 * wc) * 8);
    const u32x2 s0 = __builtin_amdgcn_raw_buffer_load_b64(rst, r32 * 8, 0, 16), s1 = __builtin_amdgcn_raw_buffer_load_b64(rst, r32 * 8, 256, 16);
    quad(BC(false), BC(true), 16);
    STAMP(1);
    __builtin_amdgcn_s_setprio(GVPRIO);
    WAITL0(); BAR();
    if (r32 == 0) {
#pragma unroll
      for (int a = 0; a < 3; ++a)
#pragma unroll
        for (int q4 = 0; q4 < 4; ++q4) *(LDS f32x4*)(sb + ((w * 2 + hf) * 3 + a) * 64 + q4 * 16) = f32x4{cs[a][4 * q4], cs[a][4 * q4 + 1], cs[a][4 * q4 + 2], cs[a][4 * q4 + 3]};
    }
    if (w == 0) flag_wait(ws, lane < 8 ? FL_WO + (j & 7) * 8 + lane : -1, -1, tag);
    WAITL0(); BAR();
    {
#pragma unroll
      for (int a = 0; a < 3; ++a)
#pragma unroll
        for (int q4 = 0; q4 < 4; ++q4) {
          const f32x4 o = *(LDS f32x4*)(sb + (((w ^ 1) * 2 + hf) * 3 + a) * 64 + q4 * 16);
          cs[a][4 * q4] += o[0]; cs[a][4 * q4 + 1] += o[1]; cs[a][4 * q4 + 2] += o[2]; cs[a][4 * q4 + 3] += o[3];
        }
#pragma unroll
      for (int tt = 0; tt < 2; ++tt) {
        const float mb = __uint_as_float(tt ? s1[0] : s0[0]);
        float rs = __uint_as_float(tt ? s1[1] : s0[1]), nm = -mb * rs;
#pragma unroll
        for (int a = 0; a < 3; ++a) {
          f32x16 v = c[2 * a + tt];
#pragma unroll
          for (int i = 0; i < 16; ++i) v[i] = v[i] * rs + cs[a][i] * nm;
          u32x4 o;
          o[0] = pk2(silu_mul(v[0], v[4]), silu_mul(v[1], v[5])); o[1] = pk2(silu_mul(v[2], v[6]), silu_mul(v[3], v[7]));
          o[2] = pk2(silu_mul(v[8], v[12]), silu_mul(v[9], v[13])); o[3] = pk2(silu_mul(v[10], v[14]), silu_mul(v[11], v[15]));
          st16_sc1(ws + WS_H + ((size_t)((T1 >> 5) + 2 * wc + tt) * 96 + (hc >> 4) + t0 + a) * 1024 + l16, o);
        }
      }
    }
    __builtin_amdgcn_s_setprio(GPRIO);
    WAITV(0);
    if (lane == 0) flag_set(ws, FL_H + (T1 >> 7) * 64 + (hc / 96) * 4 + w, tag);
  }
  STAMP(2);
  const int rb = j >> 3, g = j & 7, T0 = 256 * xcd + 64 * rb;
  BAR();
  {
    const char* wo = ws + WS_WO + (size_t)g * 40 * 2560 + w * 2560;
#pragma unroll
    for (int i = 0; i < 10; ++i) { glds_m0(sb + w * 20480 + i * 2048); glds_go_sc1<0>(wo + i * 10240, l16); glds_go_sc1<1024>(wo + i * 10240, l16); }
  }
  flag_wait(ws, FL_H + (T0 >> 7) * 64 + lane, lane < 24 ? FL_WD + g * 32 + lane : -1, tag);
  CFENCE();
  STAMP(5);
  STAMP(6);
  GAcc c;
#pragma unroll
  for (int t = 0; t < 6; ++t) c.t[t] = f32x16{};
  rs_gemm<24, 4>(c, ws + WS_H + (size_t)(T0 >> 5) * 96 * 1024, 96 * 1024, ws + WS_WD + (size_t)g * 96 * 2560, w, lane, [] {}, stp);
  STAMP(3);
  bf16x8 a2[10]; u32x2 xr[6];
  {
    const rsrc_t rw = mk_rsrc(ws + WS_WO + (size_t)g * 40 * 2560 + w * 2560);
    const unsigned l2 = 2048 + ((lane >> 5) * 16 + (lane & 15)) * 16;
#pragma unroll
    for (int i = 0; i < 10; ++i) a2[i] = bload(rw, l2, i * 10240);
#pragma unroll
    for (int a = 0; a < 3; ++a)
#pragma unroll
      for (int tt = 0; tt < 2; ++tt) {
        const int col = 80 * g + (a == 2 && w >= 2 ? 0 : 32 * a + 8 * w + 4 * hf);
        xr[2 * a + tt] = *(const u32x2*)(x + (size_t)(T0 + 32 * tt + r32) * 1280 + col * 2);
      }
  }
  if (blockIdx.x == 256 && w == 1) {
#pragma unroll 1
    for (int i = 0; i < 4; ++i) flag_wait(ws, FL_S + 128 * i + lane, FL_S + 128 * i + 64 + lane, tag);
    if (lane == 0) flag_set(ws, FL_E, tag);
  }
  flag_wait(ws, lane < 32 ? FL_O + (T0 >> 6) * 32 + lane : -1, -1, tag);
  CFENCE();
  STAMP(7);
  __builtin_amdgcn_s_setprio(GVPRIO);
  {
    const rsrc_t rx = mk_rsrc(ws + WS_O + (size_t)(T0 >> 5) * 40 * 1024 + w * 1024);
    bf16x8 b0[10], b1[10];
#pragma unroll
    for (int i = 0; i < 10; ++i) { b0[i] = bload(rx, l16, i * 4096); b1[i] = bload(rx, l16, 40 * 1024 + i * 4096); SB0(); }
#pragma unroll
    for (int i = 0; i < 10; ++i) {
      const bf16x8 a0 = ldsr128(sb + w * 20480 + i * 2048 + l16), a1 = ldsr128(sb + w * 20480 + i * 2048 + 1024 + l16);
      c.t[0] = MFMA32(a0, b0[i], c.t[0]); c.t[1] = MFMA32(a0, b1[i], c.t[1]);
      c.t[2] = MFMA32(a1, b0[i], c.t[2]); c.t[3] = MFMA32(a1, b1[i], c.t[3]);
      c.t[4] = MFMA32(a2[i], b0[i], c.t[4]); c.t[5] = MFMA32(a2[i], b1[i], c.t[5]);
    }
  }
  WAITL0(); BAR();
  f32x4 r[6];
  gemm_reduce1(c, r, sb, w, lane);
#pragma unroll
  for (int a = 0; a < 3; ++a) {
    if (a == 2 && w >= 2) continue;
#pragma unroll
    for (int tt = 0; tt < 2; ++tt) {
      const f32x4 v = r[2 * a + tt]; const u32x2 xx = xr[2 * a + tt];
      const int col = 80 * g + 32 * a + 8 * w + 4 * hf;
      *(u32x2*)(out + (size_t)(T0 + 32 * tt + r32) * 1280 + col * 2) = u32x2{pk2(bflo(xx[0]) + v[0], bfhi(xx[0]) + v[1]), pk2(bflo(xx[1]) + v[2], bfhi(xx[1]) + v[3])};
    }
  }
  STAMP(4);
}

#if defined(__HIP_DEVICE_COMPILE__)
#define NO_PK __attribute__((target("no-packed-fp32-ops")))
#else
#define NO_PK
#endif
__global__ NO_PK __launch_bounds__(256, 2) void fused(const char* __restrict__ x, const char* __restrict__ wq, const char* __restrict__ wk, const char* __restrict__ wv, const char* __restrict__ wo,
                                                const char* __restrict__ wg, const char* __restrict__ wu, const char* __restrict__ wd, char* __restrict__ ws, char* __restrict__ out) {
  extern __shared__ __attribute__((aligned(16))) char smem[];
  const unsigned sb = (unsigned)(uintptr_t)(LDS char*)smem;
  const int lane = threadIdx.x & 63, w = __builtin_amdgcn_readfirstlane(threadIdx.x >> 6);
  const int b = blockIdx.x & 255;
  Stamps stp; stamps_init(stp);
  RTSTAMP(0);
  if (blockIdx.x >= 256) {
    ffn_role(x, wg, wu, ws, out, b & 7, b >> 3, sb, w, lane, stp);
  } else {
    unsigned long long tag;
    if (b < 128) {
      const u32x2 ev = ln_kv_unit(b, x, wk, wv, ws, sb, w, lane, stp);
      tag = (((unsigned long long)__builtin_amdgcn_readfirstlane(ev[1]) << 32) | __builtin_amdgcn_readfirstlane(ev[0])) + 1ull;
      if (w == 0 && lane == 0) flag_set(ws, FL_XN + b, tag);
    } else {
      const unsigned long long e0 = __hip_atomic_load((gu64*)(ws + WS_FLAG) + FL_E, RLX_AGENT);
      tag = (((unsigned long long)__builtin_amdgcn_readfirstlane((unsigned)(e0 >> 32)) << 32) | __builtin_amdgcn_readfirstlane((unsigned)e0)) + 1ull;
      if (b < 212) {
        const int fl = retile_unit((b - 128) * 4 + w, wq, wo, wd, ws, sb + w * 13312, lane);
        if (lane == 0) flag_set(ws, fl, tag);
      }
    }
    asm volatile("" :: "s"(tag));
    RTSTAMP(1);
    BAR();
    if (w == 0 && lane == 0) flag_set(ws, FL_S + blockIdx.x, tag);
    attn_role(ws, b & 7, b >> 3, sb, w, lane, tag, stp);
  }
  RTSTAMP(3);
  stamps_flush(stp, ws);
}
extern "C" void kernel_launch(void* const* d_in, const int* in_sizes, int n_in, void* d_out, int out_size, void* d_ws, size_t ws_size, hipStream_t stream) {
  (void)hipFuncSetAttribute((const void*)fused, hipFuncAttributeMaxDynamicSharedMemorySize, 81920);
  fused<<<512, 256, 81920, stream>>>((const char*)d_in[0], (const char*)d_in[1], (const char*)d_in[2], (const char*)d_in[3], (const char*)d_in[4],
                                     (const char*)d_in[6], (const char*)d_in[7], (const char*)d_in[8], (char*)d_ws, (char*)d_out);
}
```
